```python
import torch

def _softmax(x, axis=-1):
    return torch.softmax(x, dim=axis)
def _gelu(x):
    return torch.nn.functional.gelu(x, approximate="tanh")
def _silu(x):
    return torch.nn.functional.silu(x)
def _sigmoid(x):
    return torch.sigmoid(x)
def _layernorm(x, axis=-1, eps=1e-5):
    m = x.mean(dim=axis, keepdim=True)
    v = x.var(dim=axis, keepdim=True, unbiased=False)
    return (x - m) / torch.sqrt(v + eps)
def _scatter_add(tgt, idx, src, axis=0):
    return tgt.clone().index_add_(axis, idx, src)
def _segment_sum(src, seg, n):
    out = torch.zeros((n,) + src.shape[1:], dtype=src.dtype)
    return out.index_add_(0, seg, src)
def _pad(x, pw):
    flat = tuple(v for pair in reversed(pw) for v in pair)
    return torch.nn.functional.pad(x, flat)

kernel_name = 'recurrent_scan__gqa_block_cached__glu_gated_geglu__decode'

def setup_inputs():

    _w = (lambda a: (lambda t: t.to(torch.bfloat16) if t.is_floating_point() else t)(torch.as_tensor(a)))
    def _A_setup():
        B, Sq, D = 1, 1, 1024
        Hq, Hkv, Sk, Dh = 32, 4, 65536, 128
        assert Hq % Hkv == 0, (Hq, Hkv)
        return {
            "x": _w((lambda *s: torch.randn(*s, dtype=torch.bfloat16) * 0.4)(B, Sq, D)),
            "k_cache": _w((lambda *s: torch.randn(*s, dtype=torch.bfloat16) * 1.6)(B, Hkv, Sk, Dh)),
            "v_cache": _w((lambda *s: torch.randn(*s, dtype=torch.bfloat16) * 1.6)(B, Hkv, Sk, Dh)),
            "wq": _w((lambda *s: torch.randn(*s, dtype=torch.bfloat16) * 1.6)(D, Hq * Dh) * (D ** -0.5)),
            "wo": _w((lambda *s: torch.randn(*s, dtype=torch.bfloat16) * 1.6)(Hq * Dh, D) * ((Hq * Dh) ** -0.5)),
        }
    _A_in = _A_setup()

    def _B_setup():
        B, S, Dm, Df = 1, 1, 1024, 3584
        return {
            "x": _w((lambda *s: torch.randn(*s, dtype=torch.bfloat16) * 0.4)(B, S, Dm)),
            "w_gate": _w((lambda *s: torch.randn(*s, dtype=torch.bfloat16) * 1.6)(Dm, Df) * (Dm ** -0.5)),
            "w_up": _w((lambda *s: torch.randn(*s, dtype=torch.bfloat16) * 1.6)(Dm, Df) * (Dm ** -0.5)),
            "w_down": _w((lambda *s: torch.randn(*s, dtype=torch.bfloat16) * 1.6)(Df, Dm) * (Df ** -0.5)),
        }
    _B_in = _B_setup()

    return {
        "A_x": _A_in["x"],
        "A_k_cache": _A_in["k_cache"],
        "A_v_cache": _A_in["v_cache"],
        "A_wq": _A_in["wq"],
        "A_wo": _A_in["wo"],
        "B_x": _B_in["x"],
        "B_w_gate": _B_in["w_gate"],
        "B_w_up": _B_in["w_up"],
        "B_w_down": _B_in["w_down"],
    }

def reference(A_x, A_k_cache, A_v_cache, A_wq, A_wo, B_x, B_w_gate, B_w_up, B_w_down):
    def _A_ref(x, k_cache, v_cache, wq, wo):
        B, Sq, D = x.shape
        Hq, Dh = 32, 128
        Hkv = k_cache.shape[1]
        rep = Hq // Hkv
        q = (lambda x, *d: x.permute(*d))((x @ wq).reshape(B, Sq, Hq, Dh), 0, 2, 1, 3)
        k = (lambda x, n, axis: torch.repeat_interleave(x, n, dim=axis))(k_cache, rep, axis=1)
        v = (lambda x, n, axis: torch.repeat_interleave(x, n, dim=axis))(v_cache, rep, axis=1)
        scores = torch.einsum("bhqd,bhkd->bhqk", q, k) * (Dh ** -0.5)
        p = _softmax(scores, axis=-1)
        o = torch.einsum("bhqk,bhkd->bhqd", p, v)
        return (lambda x, *d: x.permute(*d))(o, 0, 2, 1, 3).reshape(B, Sq, Hq * Dh) @ wo

    def _B_ref(x, w_gate, w_up, w_down):
        g = torch.einsum("bsd,df->bsf", x, w_gate)
        u = torch.einsum("bsd,df->bsf", x, w_up)
        h = _gelu(g) * u
        return torch.einsum("bsf,fd->bsd", h, w_down)

    x = A_x
    xn = _layernorm(x, axis=-1)
    s = _A_ref(xn, A_k_cache, A_v_cache, A_wq, A_wo)
    g = _silu(_B_ref(xn, B_w_gate, B_w_up, B_w_down))
    return x + s * g
```

```cpp
#include <hip/hip_runtime.h>
#include <hip/hip_bf16.h>

namespace {

typedef unsigned v4u __attribute__((ext_vector_type(4)));
typedef unsigned v2u __attribute__((ext_vector_type(2)));
typedef float f32x4 __attribute__((ext_vector_type(4)));
typedef float f32x2 __attribute__((ext_vector_type(2)));
typedef __bf16 bf16x8 __attribute__((ext_vector_type(8)));
typedef __bf16 bf16x2 __attribute__((ext_vector_type(2)));
#define GAS __attribute__((address_space(1)))
#define LAS __attribute__((address_space(3)))

#define KV_AUX 2
#define W_AUX 2
constexpr int AUX_SC1 = 16;

constexpr int D = 1024, HQ = 32, HKV = 4, SK = 65536, DH = 128, DF = 3584;
constexpr int NWG = 256, NTHR = 512;

constexpr int WS_E = 0;
constexpr int WS_Q1 = 4096;
constexpr int WS_Q2 = WS_Q1 + 8 * 32 * 32 * 128;
constexpr int WS_GU = WS_Q2 + 4 * 4096;
constexpr int WS_ML = WS_GU + 8 * 32 * 32 * 256;
constexpr int PSTR = 8192 + 128;
constexpr int OSTR = 16384 + 128, SSTR = 4096 + 128;
constexpr int WS_OP = WS_ML + 4 * 8 * 64 * 16;
constexpr int WS_DP = WS_OP + 4 * 32 * OSTR;
constexpr int WS_SP = WS_DP + 256 * PSTR;
constexpr int WS_END = WS_SP + 256 * SSTR;

constexpr int ws_pad_in_page(int n) {
    for (int a = WS_OP + 16384; a < WS_DP; a += OSTR) if (a >= (n << 21)) return a;
    for (int a = WS_DP + 8192; a < WS_SP; a += PSTR) if (a >= (n << 21)) return a;
    for (int a = WS_SP + 4096; a < WS_END; a += SSTR) if (a >= (n << 21)) return a;
    return WS_END - 128;
}

constexpr int L_MISC = 0;
constexpr int L_Q = 64;
constexpr int L_H = L_Q + 2048;
constexpr int L_M = L_H + 64;
constexpr int L_L = L_M + 256;
constexpr int L_R1 = L_L + 256;
constexpr int L_R2 = L_R1 + 576;
constexpr int L_BIG = L_R2 + 1024;
constexpr int L_RQ = L_BIG, L_RG = L_BIG + 16384, L_RU = L_BIG + 32768, L_O = L_BIG;
constexpr int L_END = L_BIG + 49152;

constexpr unsigned SPIN_CAP = 1u << 18;
#define POLL_SLEEP 2
#define PIPE_SLEEP 4

struct Params {
    const unsigned short* x;
    const unsigned short* k;
    const unsigned short* v;
    const unsigned short* wq;
    const unsigned short* wo;
    const unsigned short* wg;
    const unsigned short* wu;
    const unsigned short* wd;
    unsigned short* out;
    unsigned char* ws;
};

__device__ __forceinline__ __amdgpu_buffer_rsrc_t mk_rsrc(const void* p, unsigned bytes) {
    return __builtin_amdgcn_make_buffer_rsrc((void*)p, (short)0, (int)bytes, 0x00020000);
}
template <int AUX> __device__ __forceinline__ v4u bld(__amdgpu_buffer_rsrc_t r, int voff, int soff) {
    return __builtin_amdgcn_raw_buffer_load_b128(r, voff, __builtin_amdgcn_readfirstlane(soff), AUX);
}
__device__ __forceinline__ void bst128(__amdgpu_buffer_rsrc_t r, int off, v4u u) {
    __builtin_amdgcn_raw_buffer_store_b128(u, r, off, 0, AUX_SC1);
}
__device__ __forceinline__ void bst_unit(__amdgpu_buffer_rsrc_t r, int voff, int soff, float a, float b, unsigned tag) {
    bst128(r, voff + soff, v4u{__builtin_bit_cast(unsigned, a), tag, __builtin_bit_cast(unsigned, b), tag});
}
__device__ __forceinline__ float asf(unsigned u) { return __builtin_bit_cast(float, u); }
__device__ __forceinline__ unsigned asu(float f) { return __builtin_bit_cast(unsigned, f); }
__device__ __forceinline__ float bf2f(unsigned b) { return asf(b << 16); }
__device__ __forceinline__ float rbf(float f) { return (float)(__bf16)f; }
__device__ __forceinline__ unsigned pk_bf16(float lo, float hi) {
    bf16x2 v = {(__bf16)lo, (__bf16)hi};
    return __builtin_bit_cast(unsigned, v);
}
__device__ __forceinline__ float dot2(unsigned a, unsigned b, float c) {
    return __builtin_amdgcn_fdot2_f32_bf16(__builtin_bit_cast(bf16x2, a), __builtin_bit_cast(bf16x2, b), c, false);
}
__device__ __forceinline__ f32x4 mfma(v4u a, v4u b, f32x4 c) {
    return __builtin_amdgcn_mfma_f32_16x16x32_bf16(__builtin_bit_cast(bf16x8, a), __builtin_bit_cast(bf16x8, b), c, 0, 0, 0);
}
template <int CTRL> __device__ __forceinline__ float dpp(float x) {
    return asf((unsigned)__builtin_amdgcn_mov_dpp((int)asu(x), CTRL, 0xf, 0xf, true));
}
constexpr int XOR1 = 0xB1, XOR2 = 0x4E, HMIRROR = 0x141, ROR8 = 0x128, ROR4 = 0x124;
__device__ __forceinline__ float swap32_add(float a, float b) {
    auto s = __builtin_amdgcn_permlane32_swap(asu(a), asu(b), false, false);
    return asf(s[0]) + asf(s[1]);
}
__device__ __forceinline__ float swap16_add(float a, float b) {
    auto s = __builtin_amdgcn_permlane16_swap(asu(a), asu(b), false, false);
    return asf(s[0]) + asf(s[1]);
}
__device__ __forceinline__ float xrow_max(float x) {
    auto s = __builtin_amdgcn_permlane16_swap(asu(x), asu(x), false, false);
    x = fmaxf(asf(s[0]), asf(s[1]));
    auto t = __builtin_amdgcn_permlane32_swap(asu(x), asu(x), false, false);
    return fmaxf(asf(t[0]), asf(t[1]));
}
__device__ __forceinline__ float xrow_sum(float x) { x = swap16_add(x, x); return swap32_add(x, x); }
__device__ __forceinline__ float row_sum(float x) {
    x += dpp<XOR1>(x); x += dpp<XOR2>(x); x += dpp<HMIRROR>(x); x += dpp<ROR8>(x); return x;
}
__device__ __forceinline__ float row_max(float x) {
    x = fmaxf(x, dpp<XOR1>(x)); x = fmaxf(x, dpp<XOR2>(x)); x = fmaxf(x, dpp<HMIRROR>(x)); x = fmaxf(x, dpp<ROR8>(x)); return x;
}
__device__ __forceinline__ float wave_sum(float x) { return xrow_sum(row_sum(x)); }
__device__ __forceinline__ float wave_max(float x) { return xrow_max(row_max(x)); }
__device__ __forceinline__ void wg_barrier() { asm volatile("s_waitcnt lgkmcnt(0)\n\ts_barrier" ::: "memory"); }
#define TCODE() do { } while (0)
#define SB() __builtin_amdgcn_sched_barrier(0)
#define WIN 4
#define WIN_END 8
#define THRW(W, n) asm volatile("s_waitcnt vmcnt(%0)" :: "n"((W) - (n)) : "memory")
#define THR(n) THRW(WIN, n)
#define PACE_A 0
#define PACE_B 0
#define PACE(k) do { if (PACE_B > 0 && w == 0) { const unsigned due_ = tst + (unsigned)(PACE_A + (PACE_B * (k)) / 100); \
        while ((int)((unsigned)__builtin_amdgcn_s_memrealtime() - due_) < 0) __builtin_amdgcn_s_sleep(1); } \
        __builtin_amdgcn_s_barrier(); } while (0)
#define PIN4(a) asm volatile("" : "+v"(a[0]), "+v"(a[1]), "+v"(a[2]), "+v"(a[3]))
#define STAMP(i) do { } while (0)

__device__ __forceinline__ void row_dot(const v4u (&w)[4], const unsigned (&xb)[4], LAS unsigned char* dst) {
    float a[8];
#pragma unroll
    for (int e = 0; e < 8; ++e) a[e] = 0.f;
#pragma unroll
    for (int i = 0; i < 4; ++i) {
        const unsigned xl = xb[i], xh = xb[i] << 16;
#pragma unroll
        for (int d = 0; d < 4; ++d) { a[2 * d] = dot2(w[i][d], xl, a[2 * d]); a[2 * d + 1] = dot2(w[i][d], xh, a[2 * d + 1]); }
    }
    *(LAS f32x4*)dst = f32x4{a[0], a[1], a[2], a[3]};
    *(LAS f32x4*)(dst + 16) = f32x4{a[4], a[5], a[6], a[7]};
}
__device__ __forceinline__ f32x2 rows_reduce(const v4u (&w)[4], unsigned a0, unsigned a1) {
    const v4u afl = {a0, a1, 0u, 0u}, afh = {0u, 0u, a0, a1};
    float r[8];
#pragma unroll
    for (int d = 0; d < 4; ++d) {
        const v4u bf = {__builtin_amdgcn_perm(w[1][d], w[0][d], 0x05040100u), __builtin_amdgcn_perm(w[3][d], w[2][d], 0x05040100u),
                        __builtin_amdgcn_perm(w[1][d], w[0][d], 0x07060302u), __builtin_amdgcn_perm(w[3][d], w[2][d], 0x07060302u)};
        r[2 * d] = mfma(afl, bf, f32x4{0.f, 0.f, 0.f, 0.f})[0];
        r[2 * d + 1] = mfma(afh, bf, f32x4{0.f, 0.f, 0.f, 0.f})[0];
    }
    const int l = threadIdx.x;
    const bool b4 = l & 16, b5 = l & 32;
    const float e0 = b5 ? (b4 ? r[6] : r[4]) : (b4 ? r[2] : r[0]), e1 = b5 ? (b4 ? r[7] : r[5]) : (b4 ? r[3] : r[1]);
    return f32x2{e0, e1};
}
__device__ __forceinline__ void rows_pack32(const v4u (&w)[8], v4u (&bf)[8]) {
#pragma unroll
    for (int m = 0; m < 8; ++m)
#pragma unroll
        for (int jj = 0; jj < 4; ++jj) bf[m][jj] = __builtin_amdgcn_perm(w[2 * jj + 1][m >> 1], w[2 * jj][m >> 1], (m & 1) ? 0x07060302u : 0x05040100u);
}
__device__ __forceinline__ f32x2 rows_dot32(const v4u (&bf)[8], v4u af) {
    float r[8];
#pragma unroll
    for (int m = 0; m < 8; ++m) r[m] = mfma(af, bf[m], f32x4{0.f, 0.f, 0.f, 0.f})[0];
    const int l = threadIdx.x;
    const bool b4 = l & 16, b5 = l & 32;
    const float e0 = b5 ? (b4 ? r[6] : r[4]) : (b4 ? r[2] : r[0]), e1 = b5 ? (b4 ? r[7] : r[5]) : (b4 ? r[3] : r[1]);
    return f32x2{e0, e1};
}
__device__ __forceinline__ unsigned unit_bad(v4u u, unsigned tag) { return (u[1] ^ tag) | (u[3] ^ tag); }

#define POLL1(u, need, LOADEXPR) do { \
        unsigned bad_ = (need) ? unit_bad(u, tag) : 0u; \
        if (__builtin_expect(__any(bad_ != 0u), 0)) { \
            unsigned spins_ = 0; bool done_; \
            do { \
                __builtin_amdgcn_s_sleep(POLL_SLEEP); \
                asm volatile("" ::: "memory"); \
                if (bad_) u = LOADEXPR; \
                bad_ = (need) ? unit_bad(u, tag) : 0u; \
                done_ = !__any(bad_ != 0u); \
                if (++spins_ > SPIN_CAP) { timeout = true; TCODE(); done_ = true; } \
            } while (!done_); \
        } \
    } while (0)

#define POLLN(arr, N, need, LOADEXPR) do { \
        unsigned any_ = 0u; \
        _Pragma("unroll") for (int j = 0; j < (N); ++j) any_ |= unit_bad(arr[j], tag); \
        any_ = (need) ? any_ : 0u; \
        if (__builtin_expect(__any(any_ != 0u), 0)) { \
            unsigned spins_ = 0; bool done_; \
            do { \
                __builtin_amdgcn_s_sleep(POLL_SLEEP); \
                asm volatile("" ::: "memory"); \
                if (any_) { _Pragma("unroll") for (int j = 0; j < (N); ++j) arr[j] = LOADEXPR; } \
                any_ = 0u; \
                _Pragma("unroll") for (int j = 0; j < (N); ++j) any_ |= unit_bad(arr[j], tag); \
                any_ = (need) ? any_ : 0u; \
                done_ = !__any(any_ != 0u); \
                if (++spins_ > SPIN_CAP) { timeout = true; TCODE(); done_ = true; } \
            } while (!done_); \
        } \
    } while (0)

constexpr int OOB = 0x40000000;
template <int N, class LD> __device__ __forceinline__ bool poll_pipe(v4u (&u)[N], unsigned tag, int voff, LD ld) {
    unsigned bad[N], any = 0u;
#pragma unroll
    for (int j = 0; j < N; ++j) { bad[j] = unit_bad(u[j], tag); any |= bad[j]; }
    if (__builtin_expect(!__any(any != 0u), 1)) return true;
    v4u A[N], B[N];
#pragma unroll
    for (int j = 0; j < N; ++j) A[j] = ld(j, bad[j] ? voff : OOB);
    bool ok = false;
    for (unsigned spins = 0; spins <= SPIN_CAP && !ok; ++spins) {
        __builtin_amdgcn_s_sleep(PIPE_SLEEP);
        asm volatile("" ::: "memory");
#pragma unroll
        for (int j = 0; j < N; ++j) B[j] = ld(j, bad[j] ? voff : OOB);
        SB();
        any = 0u;
#pragma unroll
        for (int j = 0; j < N; ++j) { const bool take = bad[j] && !unit_bad(A[j], tag); u[j] = take ? A[j] : u[j]; bad[j] = take ? 0u : bad[j]; any |= bad[j]; }
        if (!__any(any != 0u)) { ok = true; break; }
        __builtin_amdgcn_s_sleep(PIPE_SLEEP);
        asm volatile("" ::: "memory");
#pragma unroll
        for (int j = 0; j < N; ++j) A[j] = ld(j, bad[j] ? voff : OOB);
        SB();
        any = 0u;
#pragma unroll
        for (int j = 0; j < N; ++j) { const bool take = bad[j] && !unit_bad(B[j], tag); u[j] = take ? B[j] : u[j]; bad[j] = take ? 0u : bad[j]; any |= bad[j]; }
        ok = !__any(any != 0u);
    }
    return ok;
}

__global__ void __launch_bounds__(NTHR, 2) fused_decode_kernel(Params p) {
    __shared__ __attribute__((aligned(16))) unsigned char smem[L_END];
    LAS unsigned char* L = (LAS unsigned char*)smem;
    const int tid = threadIdx.x, lane = tid & 63, w = __builtin_amdgcn_readfirstlane(tid >> 6);
    STAMP(0);
    const unsigned tst = (unsigned)__builtin_amdgcn_s_memrealtime();
    const int b = blockIdx.x, x8 = b & 7, i32 = b >> 3;
    const int g = x8 >> 1, s = (x8 & 1) * 32 + i32;
    const int c32 = 32 * x8 + i32;
    const bool fin = (i32 & 3) == 0;
    const int c16 = 8 * x8 + (i32 >> 2);
    const int hj = s >> 3;
    bool timeout = false;

    const __amdgpu_buffer_rsrc_t rX = mk_rsrc(p.x, D * 2);
    const __amdgpu_buffer_rsrc_t rWq = mk_rsrc(p.wq, D * HQ * DH * 2);
    const __amdgpu_buffer_rsrc_t rWg = mk_rsrc(p.wg, D * DF * 2);
    const __amdgpu_buffer_rsrc_t rWu = mk_rsrc(p.wu, D * DF * 2);
    const __amdgpu_buffer_rsrc_t rWd = mk_rsrc(p.wd, DF * D * 2);
    const __amdgpu_buffer_rsrc_t rWo = mk_rsrc(p.wo, HQ * DH * D * 2);
    const __amdgpu_buffer_rsrc_t rK = mk_rsrc(p.k + (size_t)g * SK * DH, SK * DH * 2);
    const __amdgpu_buffer_rsrc_t rV = mk_rsrc(p.v + (size_t)g * SK * DH, SK * DH * 2);
    const __amdgpu_buffer_rsrc_t rS = mk_rsrc(p.ws, WS_END);

    const int rowq = 32 * i32 + 4 * w;
    const v4u xa = bld<0>(rX, lane * 16, 0), xb = bld<0>(rX, lane * 16, 1024);
    const v2u xq2 = __builtin_amdgcn_raw_buffer_load_b64(rX, 0, rowq * 2, 0);
    const unsigned xres = __builtin_amdgcn_raw_buffer_load_b16(rX, (lane & 15) * 2, c16 * 32, 0);
    const unsigned ep = __builtin_amdgcn_raw_buffer_load_b32(mk_rsrc(p.ws, w == 0 ? 4 : 0), 0, WS_E, AUX_SC1);
    v4u wq_[4], wg_[4], wu_[4];
    const int sq = rowq * (HQ * DH * 2) + x8 * 1024, sf = rowq * (DF * 2) + x8 * 896;
    const int vf = lane < 56 ? lane * 16 : OOB;
#pragma unroll
    for (int i = 0; i < 4; ++i) wq_[i] = bld<W_AUX>(rWq, lane * 16, sq + i * (HQ * DH * 2));
    unsigned tw[8];
    if (w == 0) {
        const unsigned short* pv = p.v + (size_t)g * SK * DH + (size_t)s * 1024 * DH;
        const unsigned short* pd = p.wd + (size_t)14 * c32 * D;
        const unsigned short* po = p.wo + (size_t)(1024 * g + 32 * (s >> 1)) * D;
        asm volatile("s_load_dword %0, %8, 0x0\n\ts_load_dword %1, %9, 0x0\n\ts_load_dword %2, %10, 0x0\n\t"
                     "s_load_dword %3, %11, %12\n\ts_load_dword %4, %11, %13\n\ts_load_dword %5, %11, %14\n\ts_load_dword %6, %11, %15\n\ts_load_dword %7, %11, %16"
                     : "=&s"(tw[0]), "=&s"(tw[1]), "=&s"(tw[2]), "=&s"(tw[3]), "=&s"(tw[4]), "=&s"(tw[5]), "=&s"(tw[6]), "=&s"(tw[7])
                     : "s"(pv), "s"(pd), "s"(po), "s"(p.ws), "s"(ws_pad_in_page(1)), "s"(ws_pad_in_page(2)), "s"(ws_pad_in_page(3)), "s"(ws_pad_in_page(4) < WS_END - 128 ? ws_pad_in_page(4) : WS_END - 128), "s"(WS_SP + 4096)
                     : "memory");
    }
    SB();
    STAMP(1);
    unsigned xq[4];
    {
        float fx[16];
#pragma unroll
        for (int d = 0; d < 4; ++d) {
            fx[2 * d] = bf2f(xa[d] & 0xffffu); fx[2 * d + 1] = asf(xa[d] & 0xffff0000u);
            fx[8 + 2 * d] = bf2f(xb[d] & 0xffffu); fx[8 + 2 * d + 1] = asf(xb[d] & 0xffff0000u);
        }
        const float x0 = asf(__builtin_amdgcn_readfirstlane(asu(fx[0])));
        float s1 = 0.f, s2 = 0.f;
#pragma unroll
        for (int e = 0; e < 16; ++e) { const float dlt = fx[e] - x0; s1 += dlt; s2 = fmaf(dlt, dlt, s2); }
        const float e1 = wave_sum(s1) * (1.0f / D), e2 = wave_sum(s2) * (1.0f / D);
        const float var = rbf(fmaxf(fmaf(-e1, e1, e2), 0.f));
        const float mb = rbf(x0 + e1);
        const float rden = 1.0f / rbf(sqrtf(rbf(var + 1e-5f)));
#pragma unroll
        for (int i = 0; i < 2; ++i) {
            const unsigned a = pk_bf16(rbf(bf2f(xq2[i] & 0xffffu) - mb) * rden, rbf(asf(xq2[i] & 0xffff0000u) - mb) * rden);
            xq[2 * i] = a & 0xffffu; xq[2 * i + 1] = a >> 16;
        }
    }
    if (tid == 0) { *(volatile LAS unsigned*)(L + L_MISC) = ep + 1u; *(volatile LAS unsigned*)(L + L_MISC + 4) = 0u; }
    SB();
    STAMP(2);
    const int kvo = ((lane & 15) >> 1) * 256 + (lane & 1) * 64 + (lane >> 4) * 16;
    const int kbase = (s * 1024 + w * 128) * 256, rot = 2 * w;
    v4u kf[16][2];
#define TILE_OFF(T) (kbase + (((T) + rot) & 15) * 2048)
#define LOAD_K(T) do { THR(2); kf[T][0] = bld<KV_AUX>(rK, kvo, TILE_OFF(T)); kf[T][1] = bld<KV_AUX>(rK, kvo, TILE_OFF(T) + 128); } while (0)
#pragma unroll
    for (int i = 0; i < 4; ++i) { THR(1); wg_[i] = bld<W_AUX>(rWg, vf, sf + i * (DF * 2)); }
    SB();
    row_dot(wq_, xq, L + L_RQ + w * 2048 + lane * 32);
    SB();
#pragma unroll
    for (int i = 0; i < 4; ++i) { THR(1); wu_[i] = bld<W_AUX>(rWu, vf, sf + i * (DF * 2)); }
    SB();
    row_dot(wg_, xq, L + L_RG + w * 2048 + lane * 32);
    SB();
    LOAD_K(0); LOAD_K(1);
    SB();
    row_dot(wu_, xq, L + L_RU + w * 2048 + lane * 32);
    STAMP(3);
    wg_barrier();
    if (w == 0) asm volatile("" :: "s"(tw[0]), "s"(tw[1]), "s"(tw[2]), "s"(tw[3]), "s"(tw[4]), "s"(tw[5]), "s"(tw[6]), "s"(tw[7]));
    const unsigned tag = __builtin_amdgcn_readfirstlane(*(volatile LAS unsigned*)(L + L_MISC));
    if (w < 4) {
        float a0 = 0.f, a1 = 0.f;
#pragma unroll
        for (int ww = 0; ww < 8; ++ww) { const f32x2 t = *(LAS f32x2*)(L + L_RQ + ww * 2048 + tid * 8); a0 += t[0]; a1 += t[1]; }
        bst_unit(rS, (tid >> 3) * 4096 + (tid & 7) * 16, WS_Q1 + (x8 * 1024 + i32) * 128, a0, a1, tag);
    }
    if (w < 7) {
        float a0 = 0.f, a1 = 0.f;
#pragma unroll
        for (int ww = 0; ww < 8; ++ww) { a0 += ((LAS float*)(L + L_RG))[ww * 512 + tid]; a1 += ((LAS float*)(L + L_RU))[ww * 512 + tid]; }
        const int cons = tid / 14;
        bst_unit(rS, cons * 8192 + (tid - 14 * cons) * 16, WS_GU + (x8 * 1024 + i32) * 256, a0, a1, tag);
    }
    SB();
    STAMP(4);
    LOAD_K(2); LOAD_K(3);
    PACE(20);
    LOAD_K(4); LOAD_K(5);
    PACE(24);
    SB();
    const bool hq = w == 0, hg = w == 5;
    const __amdgpu_buffer_rsrc_t rH = mk_rsrc(p.ws, (hq || hg) ? WS_END : 0), rH2 = mk_rsrc(p.ws, hg ? WS_END : 0);
    const int hso = hq ? WS_Q1 + c32 * 4096 : WS_GU + c32 * 8192;
    const bool hneed = hq || (lane & 15) < 14;
    const int hvo = hneed ? lane * 16 : OOB;
    v4u hpa[4], hpb[4];
#pragma unroll
    for (int j = 0; j < 4; ++j) hpa[j] = bld<AUX_SC1>(rH, hvo, hso + j * 1024);
#pragma unroll
    for (int j = 0; j < 4; ++j) hpb[j] = bld<AUX_SC1>(rH2, hvo, hso + 4096 + j * 1024);
    SB();
    LOAD_K(6); LOAD_K(7);
    SB();
    if (hq || hg) {
        POLLN(hpa, 4, hneed, bld<AUX_SC1>(rH, hvo, hso + j * 1024));
        if (hg) POLLN(hpb, 4, hneed, bld<AUX_SC1>(rH2, hvo, hso + 4096 + j * 1024));
        float v0 = 0.f, v1 = 0.f;
#pragma unroll
        for (int j = 0; j < 4; ++j) { v0 += asf(hpa[j][0]) + asf(hpb[j][0]); v1 += asf(hpa[j][2]) + asf(hpb[j][2]); }
        if (hq) {
            float t = swap32_add(v0, v1);
            t = swap16_add(t, t);
            t += dpp<ROR8>(t);
            auto sw = __builtin_amdgcn_permlane32_swap(asu(t), asu(t), false, false);
            const unsigned pk = pk_bf16(asf(sw[0]), asf(sw[1]));
            const unsigned pk2 = (unsigned)__builtin_amdgcn_mov_dpp((int)pk, XOR1, 0xf, 0xf, true);
            if (lane < 8 && (lane & 1) == 0) {
                bst128(rS, (lane >> 1) * 16 + WS_Q2 + g * 4096 + ((x8 & 1) * 128 + i32 * 4) * 16, v4u{pk, tag, pk2, tag});
            }
        } else {
            const float gg = rbf(xrow_sum(v0)), uu = rbf(xrow_sum(v1));
            const float y = 0.7978845608028654f * (gg + 0.044715f * gg * gg * gg);
            const float th = 1.0f - 2.0f * __builtin_amdgcn_rcpf(__expf(2.0f * y) + 1.0f);
            const float hv = rbf(0.5f * gg * (1.0f + th)) * uu;
            if (lane < 16) ((LAS unsigned*)(L + L_H))[lane] = lane < 14 ? pk_bf16(hv, 0.f) & 0xffffu : 0u;
        }
    }
    SB();
    STAMP(5);
    PACE(28);
    LOAD_K(8); LOAD_K(9);
    PACE(32);
    LOAD_K(10); LOAD_K(11);
    PACE(36);
    SB();
    v4u qg = bld<AUX_SC1>(mk_rsrc(p.ws, w < 4 ? WS_END : 0), tid * 16, WS_Q2 + g * 4096);
    SB();
    LOAD_K(12); LOAD_K(13);
    SB();
    if (w < 4) {
        POLL1(qg, true, bld<AUX_SC1>(rS, tid * 16, WS_Q2 + g * 4096));
        *(LAS v2u*)(L + L_Q + tid * 8) = v2u{qg[0], qg[2]};
    }
    SB();
    v4u wd_[4];
    const int wvo = (lane >> 4) * 2048 + (lane & 15) * 16;
    const int r4 = (i32 + (x8 >> 1)) & 3;
    LOAD_K(14); LOAD_K(15);
#pragma unroll
    for (int i = 0; i < 4; ++i) { const int rg = (i + r4) & 3; THR(1); wd_[i] = bld<W_AUX>(rWd, (rg == 3 && lane >= 32) ? OOB : wvo, (14 * c32 + 4 * rg) * 2048 + w * 256); }
    SB();
    wg_barrier();
    STAMP(6);
    const LAS unsigned* Hh = (const LAS unsigned*)(L + L_H) + (lane >> 4);
    const unsigned ha0 = Hh[4 * r4] | (Hh[4 * ((r4 + 1) & 3)] << 16), ha1 = Hh[4 * ((r4 + 2) & 3)] | (Hh[4 * ((r4 + 3) & 3)] << 16);
    const int qo = L_Q + (lane & 7) * 256 + ((lane >> 3) & 1) * 64 + (lane >> 4) * 16;
    const v4u qf0 = *(LAS v4u*)(L + qo), qf1 = *(LAS v4u*)(L + qo + 128);

    const int vvo = (lane >> 4) * 512 + (lane & 15) * 16;
    v4u vf_[4][8];
#define LOAD_V(T) do { THRW((T) < 8 ? WIN : WIN_END, 2); vf_[(T) >> 2][2 * ((T) & 3)] = bld<KV_AUX>(rV, vvo, TILE_OFF(T)); vf_[(T) >> 2][2 * ((T) & 3) + 1] = bld<KV_AUX>(rV, vvo, TILE_OFF(T) + 256); } while (0)
    float S[16][2];
    const bool lowhalf = (lane & 8) == 0;
    constexpr float SCALE = 0.08838834764831845f, LOG2E = 1.4426950408889634f;
#pragma unroll
    for (int T = 0; T < 16; ++T) {
        f32x4 dd = {0.f, 0.f, 0.f, 0.f};
        dd = mfma(kf[T][0], qf0, dd);
        dd = mfma(kf[T][1], qf1, dd);
        SB();
        if (T < 8) { LOAD_V(T); }
        if (T == 7) {
            PIN4(wd_);
            const f32x2 r = rows_reduce(wd_, ha0, ha1);
            bst_unit(rS, (4 * (lane & 15) + (lane >> 4)) * 16, WS_DP + b * PSTR + w * 1024, r[0], r[1], tag);
        }
        SB();
        const float t0 = lowhalf ? dd[0] : dd[1], t1 = lowhalf ? dd[2] : dd[3];
        const float s0 = t0 + dpp<ROR8>(t0), s1 = t1 + dpp<ROR8>(t1);
        const unsigned r1 = pk_bf16(s0, s1);
        const unsigned r2 = pk_bf16(bf2f(r1 & 0xffffu) * SCALE, asf(r1 & 0xffff0000u) * SCALE);
        S[T][0] = bf2f(r2 & 0xffffu); S[T][1] = asf(r2 & 0xffff0000u);
        if (T < 8) PACE(50 + 2 * T);
        SB();
    }
    STAMP(7);
    float mx = fmaxf(S[0][0], S[0][1]);
#pragma unroll
    for (int T = 1; T < 16; ++T) mx = fmaxf(mx, fmaxf(S[T][0], S[T][1]));
    mx = xrow_max(mx);
    if (lane < 8) ((LAS float*)(L + L_M))[lane * 8 + w] = mx;
    LOAD_V(8); LOAD_V(9);
    wg_barrier();
    {
        const f32x4 m0 = *(LAS f32x4*)(L + L_M + (lane & 7) * 32), m1 = *(LAS f32x4*)(L + L_M + (lane & 7) * 32 + 16);
        mx = fmaxf(fmaxf(fmaxf(m0[0], m0[1]), fmaxf(m0[2], m0[3])), fmaxf(fmaxf(m1[0], m1[1]), fmaxf(m1[2], m1[3])));
    }
    const float mc = mx * LOG2E;
    float lsum = 0.f;
    v4u pf[4];
#pragma unroll
    for (int T = 0; T < 16; ++T) {
        const float p0 = __builtin_amdgcn_exp2f(fmaf(S[T][0], LOG2E, -mc)), p1 = __builtin_amdgcn_exp2f(fmaf(S[T][1], LOG2E, -mc));
        lsum += p0 + p1;
        pf[T >> 2][T & 3] = pk_bf16(p0, p1);
    }
    lsum = xrow_sum(lsum);
    if (lane < 8) ((LAS float*)(L + L_L))[lane * 8 + w] = lsum;
    SB();
    STAMP(8);
    LOAD_V(10); LOAD_V(11);
    SB();
    f32x4 acc[8];
#pragma unroll
    for (int m = 0; m < 8; ++m) acc[m] = f32x4{0.f, 0.f, 0.f, 0.f};
#define PV_GROUP(G) do { \
        _Pragma("unroll") for (int m = 0; m < 8; ++m) { \
            v4u bf; \
            _Pragma("unroll") for (int jj = 0; jj < 4; ++jj) \
                bf[jj] = __builtin_amdgcn_perm(vf_[G][2 * jj + 1][m >> 1], vf_[G][2 * jj][m >> 1], (m & 1) ? 0x07060302u : 0x05040100u); \
            acc[m] = mfma(pf[G], bf, acc[m]); \
        } \
        SB(); } while (0)
    wg_barrier();
    {
        const f32x4 l0 = *(LAS f32x4*)(L + L_L + w * 32), l1 = *(LAS f32x4*)(L + L_L + w * 32 + 16);
        const float Ls = ((l0[0] + l0[1]) + (l0[2] + l0[3])) + ((l1[0] + l1[1]) + (l1[2] + l1[3]));
        const float Mw = asf(__builtin_amdgcn_readlane(asu(mx), w));
        if (lane == 0) bst_unit(rS, 0, WS_ML + ((g * 8 + w) * 64 + s) * 16, Mw, Ls, tag);
    }
    SB();
    PV_GROUP(0);
    LOAD_V(12); LOAD_V(13);
    PACE(76);
    LOAD_V(14); LOAD_V(15);
    __builtin_amdgcn_s_barrier();
    SB();
    PV_GROUP(1);
    PV_GROUP(2);
    PV_GROUP(3);
    {
        const int h2 = lane >> 4, c = lane & 15;
        const bool hi = h2 >= 2;
#pragma unroll
        for (int r = 0; r < 4; ++r) {
            f32x4 t;
#pragma unroll
            for (int m = 0; m < 4; ++m) t[m] = hi ? acc[4 + m][r] : acc[m][r];
            *(LAS f32x4*)(L + L_O + w * 4096 + ((h2 & 1) * 4 + r) * 512 + c * 32 + (hi ? 16 : 0)) = t;
        }
    }
    STAMP(9);
    wg_barrier();
    {
        f32x2 a = *(LAS f32x2*)(L + L_O + w * 512 + lane * 8);
#pragma unroll
        for (int ww = 1; ww < 8; ++ww) a += *(LAS f32x2*)(L + L_O + ww * 4096 + w * 512 + lane * 8);
        bst_unit(rS, (lane >> 4) * OSTR + (lane & 15) * 16, WS_OP + (g * 32 + 4 * w) * OSTR + s * 256, a[0], a[1], tag);
    }
    SB();
    STAMP(10);
    const int sp = s >> 1, ch = s & 1;
    const int fo = (tid >> 3) * PSTR + (tid & 7) * 16;
    const int r8 = (i32 + 3 * x8) & 7;
    const int vv = 2 * (lane & 15) + (lane >> 5);
    const int r1w = L_R1 + (4 * (((vv >> 2) - r8) & 7) + (vv & 3)) * 16 + w * 4, r1r = L_R1 + (lane >> 4) * 16;
    v4u wob[8], du[8];
    if (w < 4) {
        __builtin_amdgcn_s_barrier();
        v4u ml = bld<AUX_SC1>(rS, lane * 16, WS_ML + (g * 8 + hj) * 1024);
        v4u og[4];
#pragma unroll
        for (int j = 0; j < 4; ++j) og[j] = bld<AUX_SC1>(rS, tid * 16, WS_OP + (g * 32 + sp) * OSTR + j * 4096);
        STAMP(11);
        POLL1(ml, true, bld<AUX_SC1>(rS, lane * 16, WS_ML + (g * 8 + hj) * 1024));
        const float Ms = asf(ml[0]);
        const float e = __builtin_amdgcn_exp2f((Ms - wave_max(Ms)) * LOG2E);
        const float rL = __builtin_amdgcn_rcpf(wave_sum(e * asf(ml[2])));
        float ej[4];
#pragma unroll
        for (int j = 0; j < 4; ++j) ej[j] = asf((unsigned)__builtin_amdgcn_ds_bpermute((16 * j + 4 * w + (lane >> 4)) * 4, (int)asu(e)));
        if (!poll_pipe(og, tag, tid * 16, [&](int j, int vo) { return bld<AUX_SC1>(rS, vo, WS_OP + (g * 32 + sp) * OSTR + j * 4096); })) { timeout = true; TCODE(); }
        STAMP(12);
        float v0 = ej[0] * asf(og[0][0]), v1 = ej[0] * asf(og[0][2]);
#pragma unroll
        for (int j = 1; j < 4; ++j) { v0 = fmaf(ej[j], asf(og[j][0]), v0); v1 = fmaf(ej[j], asf(og[j][2]), v1); }
        float t = swap32_add(v0, v1);
        t = swap16_add(t, t);
        if ((lane & 16) == 0) *(LAS float*)(L + r1w) = t;
        if (lane == 0 && w == 0) ((LAS float*)(L + L_R1))[128] = rL;
    } else {
        v4u wo8[8];
#pragma unroll
        for (int i = 0; i < 8; ++i) wo8[i] = bld<W_AUX>(rWo, wvo, (1024 * g + 32 * sp + 4 * ((i + r8) & 7)) * 2048 + ch * 1024 + (w - 4) * 256);
        __builtin_amdgcn_s_barrier();
        if (fin) {
#pragma unroll
            for (int j = 0; j < 8; ++j) du[j] = bld<AUX_SC1>(rS, fo - 32 * PSTR, WS_DP + j * 32 * PSTR + c16 * 128);
        }
        STAMP(11);
        SB();
        rows_pack32(wo8, wob);
        asm volatile("" : "+v"(wob[0]), "+v"(wob[1]), "+v"(wob[2]), "+v"(wob[3]), "+v"(wob[4]), "+v"(wob[5]), "+v"(wob[6]), "+v"(wob[7]));
    }
    if (__any(timeout) && lane == 0) *(volatile LAS unsigned*)(L + L_MISC + 4) = 1u;
    wg_barrier();
    if (w >= 4) {
        float ov[8];
        const float rl = ((LAS float*)(L + L_R1))[128];
#pragma unroll
        for (int j = 0; j < 8; ++j) {
            const f32x4 r0 = *(LAS f32x4*)(L + r1r + j * 64);
            const f32x2 t = f32x2{r0[0], r0[1]} + f32x2{r0[2], r0[3]};
            ov[j] = (t[0] + t[1]) * rl;
        }
        if (*(volatile LAS unsigned*)(L + L_MISC + 4) != 0u) ov[0] = __builtin_nanf("");
        const f32x2 r = rows_dot32(wob, v4u{pk_bf16(ov[0], ov[1]), pk_bf16(ov[2], ov[3]), pk_bf16(ov[4], ov[5]), pk_bf16(ov[6], ov[7])});
        bst_unit(rS, (4 * (lane & 15) + (lane >> 4)) * 16, WS_SP + (ch * 128 + g * 32 + sp) * SSTR + (w - 4) * 1024, r[0], r[1], tag);
    }
    SB();
    STAMP(13);
    if (fin) {
        SB();
        if (w >= 4) {
            POLLN(du, 8, true, bld<AUX_SC1>(rS, fo - 32 * PSTR, WS_DP + j * 32 * PSTR + c16 * 128));
            float d0 = 0.f, d1 = 0.f;
#pragma unroll
            for (int j = 0; j < 8; ++j) { d0 += asf(du[j][0]); d1 += asf(du[j][2]); }
            float t = swap32_add(d0, d1);
            t = swap16_add(t, t);
            t += dpp<ROR8>(t);
            if ((lane & 24) == 0) ((LAS float*)(L + L_R2))[128 + (2 * (lane & 7) + (lane >> 5)) * 4 + (w - 4)] = t;
        }
        wg_barrier();
        v4u su[2];
        const int so = (tid >> 3) * SSTR + (tid & 7) * 16, sb = WS_SP + (c16 >> 5) * 128 * SSTR + (c16 & 31) * 128;
#pragma unroll
        for (int j = 0; j < 2; ++j) su[j] = bld<AUX_SC1>(rS, so, sb + j * 64 * SSTR);
        SB();
        float gt = 0.f;
        if (w == 7 && lane < 16) {
            const f32x4 c0 = *(LAS f32x4*)(L + L_R2 + 512 + lane * 16);
            const float dv = rbf((c0[0] + c0[1]) + (c0[2] + c0[3]));
            gt = rbf(dv * __builtin_amdgcn_rcpf(1.0f + __expf(-dv)));
        }
        if (!poll_pipe(su, tag, so, [&](int j, int vo) { return bld<AUX_SC1>(rS, vo, sb + j * 64 * SSTR); })) { timeout = true; TCODE(); }
        STAMP(14);
        float t = swap32_add(asf(su[0][0]) + asf(su[1][0]), asf(su[0][2]) + asf(su[1][2]));
        t = swap16_add(t, t);
        t += dpp<ROR8>(t);
        if ((lane & 24) == 0) ((LAS float*)(L + L_R2))[(2 * (lane & 7) + (lane >> 5)) * 8 + w] = t;
        if (__any(timeout) && lane == 0) *(volatile LAS unsigned*)(L + L_MISC + 4) = 1u;
        wg_barrier();
        if (w == 7) {
            if (lane < 16) {
                const f32x4 a0 = *(LAS f32x4*)(L + L_R2 + lane * 32), a1 = *(LAS f32x4*)(L + L_R2 + lane * 32 + 16);
                const float sv = rbf(((a0[0] + a0[1]) + (a0[2] + a0[3])) + ((a1[0] + a1[1]) + (a1[2] + a1[3])));
                float o = bf2f(xres) + rbf(sv * gt);
                if (*(volatile LAS unsigned*)(L + L_MISC + 4) != 0u) o = __builtin_nanf("");
                p.out[16 * c16 + lane] = (unsigned short)(pk_bf16(o, 0.f) & 0xffffu);
            }
            if (b == 0 && lane == 0) __builtin_amdgcn_raw_buffer_store_b32(tag, rS, 0, WS_E, AUX_SC1);
        }
    }
    STAMP(15);
}

}

extern "C" void kernel_launch(void* const* d_in, const int* in_sizes, int n_in, void* d_out, int out_size, void* d_ws, size_t ws_size,
                              hipStream_t stream) {
    Params p;
    p.x = (const unsigned short*)d_in[0];
    p.k = (const unsigned short*)d_in[1];
    p.v = (const unsigned short*)d_in[2];
    p.wq = (const unsigned short*)d_in[3];
    p.wo = (const unsigned short*)d_in[4];
    p.wg = (const unsigned short*)d_in[6];
    p.wu = (const unsigned short*)d_in[7];
    p.wd = (const unsigned short*)d_in[8];
    p.out = (unsigned short*)d_out;
    p.ws = (unsigned char*)d_ws;
    hipLaunchKernelGGL(fused_decode_kernel, dim3(NWG), dim3(NTHR), 0, stream, p);
}
```
